```python
import torch

def _softmax(x, axis=-1):
    return torch.softmax(x, dim=axis)
def _gelu(x):
    return torch.nn.functional.gelu(x, approximate="tanh")
def _silu(x):
    return torch.nn.functional.silu(x)
def _sigmoid(x):
    return torch.sigmoid(x)
def _layernorm(x, axis=-1, eps=1e-5):
    m = x.mean(dim=axis, keepdim=True)
    v = x.var(dim=axis, keepdim=True, unbiased=False)
    return (x - m) / torch.sqrt(v + eps)
def _scatter_add(tgt, idx, src, axis=0):
    return tgt.clone().index_add_(axis, idx, src)
def _segment_sum(src, seg, n):
    out = torch.zeros((n,) + src.shape[1:], dtype=src.dtype)
    return out.index_add_(0, seg, src)
def _pad(x, pw):
    flat = tuple(v for pair in reversed(pw) for v in pair)
    return torch.nn.functional.pad(x, flat)

kernel_name = 'sequential_preLN__mha_block_cached__dense_activation_silu__decode'

def setup_inputs():

    _w = (lambda a: (lambda t: t.to(torch.bfloat16) if t.is_floating_point() else t)(torch.as_tensor(a)))
    def _A_setup():
        B, Sq, D = 2, 1, 640
        H, Sk, Dh = 32, 8192, 192
        return {
            "x": _w((lambda *s: torch.randn(*s, dtype=torch.bfloat16) * 0.05)(B, Sq, D)),
            "k_cache": _w((lambda *s: torch.randn(*s, dtype=torch.bfloat16) * 3.2)(B, H, Sk, Dh)),
            "v_cache": _w((lambda *s: torch.randn(*s, dtype=torch.bfloat16) * 0.8)(B, H, Sk, Dh)),
            "wq": _w((lambda *s: torch.randn(*s, dtype=torch.bfloat16) * 3.2)(D, H * Dh) * (D ** -0.5)),
            "wo": _w((lambda *s: torch.randn(*s, dtype=torch.bfloat16) * 0.8)(H * Dh, D) * ((H * Dh) ** -0.5)),
        }
    _A_in = _A_setup()

    def _B_setup():
        B, S, D, F = 2, 1, 640, 1536
        return {
            "x": _w((lambda *s: torch.randn(*s, dtype=torch.bfloat16) * 0.05)(B, S, D)),
            "w1": _w((lambda *s: torch.randn(*s, dtype=torch.bfloat16) * 3.2)(D, F) * (D ** -0.5)),
            "w2": _w((lambda *s: torch.randn(*s, dtype=torch.bfloat16) * 0.8)(F, D) * (F ** -0.5)),
        }
    _B_in = _B_setup()

    return {
        "A_x": _A_in["x"],
        "A_k_cache": _A_in["k_cache"],
        "A_v_cache": _A_in["v_cache"],
        "A_wq": _A_in["wq"],
        "A_wo": _A_in["wo"],
        "B_x": _B_in["x"],
        "B_w1": _B_in["w1"],
        "B_w2": _B_in["w2"],
    }

def reference(A_x, A_k_cache, A_v_cache, A_wq, A_wo, B_x, B_w1, B_w2):
    def _A_ref(x, k_cache, v_cache, wq, wo):
        B, Sq, D = x.shape
        H, Dh = 32, 192
        q = (lambda x, *d: x.permute(*d))((x @ wq).reshape(B, Sq, H, Dh), 0, 2, 1, 3)
        scores = torch.einsum("bhqd,bhkd->bhqk", q, k_cache) * (Dh ** -0.5)
        p = _softmax(scores, axis=-1)
        o = torch.einsum("bhqk,bhkd->bhqd", p, v_cache)
        return (lambda x, *d: x.permute(*d))(o, 0, 2, 1, 3).reshape(B, Sq, H * Dh) @ wo

    def _B_ref(x, w1, w2):
        h = torch.einsum("bsd,df->bsf", x, w1)
        h = _silu(h)
        out = torch.einsum("bsf,fd->bsd", h, w2)
        return out

    x = A_x
    h = x + _A_ref(_layernorm(x, axis=-1), A_k_cache, A_v_cache, A_wq, A_wo)
    h2 = h + _B_ref(_layernorm(h, axis=-1), B_w1, B_w2)
    return h2
```

```cpp
#include <hip/hip_runtime.h>
#include <hip/hip_bf16.h>
#include <stdint.h>

#define GAS __attribute__((address_space(1)))
#define LAS __attribute__((address_space(3)))
typedef unsigned u32;
typedef unsigned long long u64;
typedef unsigned short u16;
typedef u32 u32x4 __attribute__((ext_vector_type(4)));
typedef u32 u32x2 __attribute__((ext_vector_type(2)));
typedef int i32x2 __attribute__((ext_vector_type(2)));
typedef int i32x4 __attribute__((ext_vector_type(4)));
typedef float f32x2 __attribute__((ext_vector_type(2)));
typedef float f32x4 __attribute__((ext_vector_type(4)));
typedef __bf16 bf16x2 __attribute__((ext_vector_type(2)));
typedef __bf16 bf16x8 __attribute__((ext_vector_type(8)));
typedef short s16x4 __attribute__((ext_vector_type(4)));

constexpr int PB = 9;
constexpr int SDELAY = 32;
constexpr int CK0 = 16, CKS = 21, NCK = 5;
constexpr int CKWAIT = 500;
constexpr int AGG = 254;
constexpr int DSH = 5;
constexpr int TRIG = 121;
constexpr int PACE = 64;
constexpr int QSLEEP0 = 120, QSLEEP = 28;
constexpr int NEAR = 1, FAR = 6;
constexpr int D3 = 44, D4 = 32, D5 = 24;

namespace fk {
constexpr int D = 640, H = 32, SK = 8192, F = 1536, HD = H * 192;
constexpr int NWG = 256, NT = 768;
constexpr int WS_EPOCH = 0;
constexpr int WS_QA = 4096;
constexpr int WS_ML = WS_QA + 192 * 1024;
constexpr int WS_PA = WS_ML + NWG * 128;
constexpr int WS_PY = WS_PA + NWG * 3072;
constexpr int WS_PH = WS_PY + H * D * 16;
constexpr int WS_PS = WS_PH + 6144;
constexpr int WS_PZ = WS_PH + 12288;
constexpr int WS_CK = WS_PZ + 24 * D * 16;
constexpr int WS_END = WS_CK + 8 * 512;
constexpr u32 SPIN_CAP = 1u << 17, LDS_CAP = 1u << 23;

__device__ __forceinline__ float bits2f(u32 v) { return __builtin_bit_cast(float, v); }
__device__ __forceinline__ u32 f2bits(float v) { return __builtin_bit_cast(u32, v); }
__device__ __forceinline__ float bflo(u32 v) { return bits2f(v << 16); }
__device__ __forceinline__ float bfhi(u32 v) { return bits2f(v & 0xffff0000u); }
__device__ __forceinline__ u32 pk_bf16(float lo, float hi) {
  f32x2 v = {lo, hi};
  return __builtin_bit_cast(u32, __builtin_convertvector(v, bf16x2));
}
__device__ __forceinline__ float rbf(float f) { return bflo(pk_bf16(f, f)); }
__device__ __forceinline__ float dot2(u32 a, u32 b, float c) {
  return __builtin_amdgcn_fdot2_f32_bf16(__builtin_bit_cast(bf16x2, a), __builtin_bit_cast(bf16x2, b), c, false);
}
__device__ __forceinline__ f32x4 mfma(u32x4 a, u32x4 b, f32x4 c) {
  return __builtin_amdgcn_mfma_f32_16x16x32_bf16(__builtin_bit_cast(bf16x8, a), __builtin_bit_cast(bf16x8, b), c, 0, 0, 0);
}
template <int CTRL> __device__ __forceinline__ float dpp(float x) {
  return __builtin_bit_cast(float, __builtin_amdgcn_mov_dpp(__builtin_bit_cast(int, x), CTRL, 0xf, 0xf, true));
}
constexpr int XOR1 = 0xB1, XOR2 = 0x4E, HMIR = 0x141, ROR8 = 0x128, ROR4 = 0x124;
__device__ __forceinline__ float tr32(float a, float b) {
  auto s = __builtin_amdgcn_permlane32_swap(f2bits(a), f2bits(b), false, false);
  return bits2f(s[0]) + bits2f(s[1]);
}
__device__ __forceinline__ float tr16(float a, float b) {
  auto s = __builtin_amdgcn_permlane16_swap(f2bits(a), f2bits(b), false, false);
  return bits2f(s[0]) + bits2f(s[1]);
}
__device__ __forceinline__ float sel(bool c, float a, float b) { asm volatile("" : "+v"(a), "+v"(b)); return c ? a : b; }
__device__ __forceinline__ float max32(float a) { auto s = __builtin_amdgcn_permlane32_swap(f2bits(a), f2bits(a), false, false); return fmaxf(bits2f(s[0]), bits2f(s[1])); }
__device__ __forceinline__ float max16(float a) { auto s = __builtin_amdgcn_permlane16_swap(f2bits(a), f2bits(a), false, false); return fmaxf(bits2f(s[0]), bits2f(s[1])); }
__device__ __forceinline__ float sum8lo(float v) { v += dpp<XOR1>(v); v += dpp<XOR2>(v); v += dpp<HMIR>(v); return v; }
__device__ __forceinline__ float max8lo(float v) { v = fmaxf(v, dpp<XOR1>(v)); v = fmaxf(v, dpp<XOR2>(v)); v = fmaxf(v, dpp<HMIR>(v)); return v; }
__device__ __forceinline__ float wave_sum(float v) { v = sum8lo(v); v += dpp<ROR8>(v); v = tr16(v, v); v = tr32(v, v); return v; }
__device__ __forceinline__ float wave_max(float v) { v = max8lo(v); v = fmaxf(v, dpp<ROR8>(v)); v = max16(v); v = max32(v); return v; }
__device__ __forceinline__ float rdlane(float v, int l) { return bits2f(__builtin_amdgcn_readlane(f2bits(v), l)); }
__device__ __forceinline__ u32x4 ldnt(const GAS void* p) { return __builtin_nontemporal_load((const GAS u32x4*)p); }
#define CBAR() asm volatile("" ::: "memory")
#define SB() __builtin_amdgcn_sched_barrier(0)
#define SLEEP(n) __builtin_amdgcn_s_sleep(n)
#define LDSC1(off) __builtin_amdgcn_raw_buffer_load_b128(rs, (off), 0, 16)
#define STSC1(v0, v1, off) __builtin_amdgcn_raw_buffer_store_b128(u32x4{(v0), tag, (v1), tag}, rs, (off), 0, 16)
#define BADTAG(g) (((g)[1] ^ tag) | ((g)[3] ^ tag))

constexpr int ACT_S = 672;
struct Lds {
  float red[4][2][192];
  float redq[5][2][64];
  float sc[4][2][256];
  u16 q[2][192];
  float mx[2][4], ls[2][4];
  float gml[4];
  float st[4];
  u32 cnt[16];
  int tck[8][4];
  int sched[4];
  u16 act[3][ACT_S];
  u16 act2[2][96];
  f32x2 r3[8][16];
  u32x4 wimg[5120];
};
enum { C_QP, C_Q, C_ML, C_GML, C_TRIG, C_W2, C_ST, C_S3, C_S5, C_V0, C_H, C_V1, C_TAG };

__device__ __forceinline__ u32 lds_arrive(u32* c) { return __hip_atomic_fetch_add(c, 1u, __ATOMIC_ACQ_REL, __HIP_MEMORY_SCOPE_WORKGROUP); }
template <int SL> __device__ __forceinline__ void lds_wait(u32* c, u32 n) {
  u32 spins = 0;
#pragma nounroll
  while (__hip_atomic_load(c, __ATOMIC_ACQUIRE, __HIP_MEMORY_SCOPE_WORKGROUP) < n && ++spins < LDS_CAP) SLEEP(SL);
}
__device__ __forceinline__ u32 lds_add_relaxed(u32* c, int lane) {
  u32 z = 0, old = 0; asm volatile("" : "+v"(z));
  CBAR();
  if (lane == 0) old = __hip_atomic_fetch_add(c + z, 1u, __ATOMIC_RELAXED, __HIP_MEMORY_SCOPE_WORKGROUP);
  CBAR();
  return old;
}
#define PIN(x) asm volatile("" : "+v"(x))
#define MISS(g) (__builtin_amdgcn_ballot_w64((g)[1] != tag) | __builtin_amdgcn_ballot_w64((g)[3] != tag))
#define POLL1(g, off) do { _Pragma("nounroll") for (u32 n_ = SPIN_CAP; n_; --n_) { CBAR(); g = LDSC1(off); if (MISS(g) == 0) break; SLEEP(NEAR); } } while (0)
__device__ __forceinline__ u32x4 bfrag(const unsigned char* p, int rs4) {
  const u32x2 lo = __builtin_bit_cast(u32x2, __builtin_amdgcn_ds_read_tr16_b64_v4i16((LAS s16x4*)p));
  const u32x2 hi = __builtin_bit_cast(u32x2, __builtin_amdgcn_ds_read_tr16_b64_v4i16((LAS s16x4*)(p + rs4)));
  return u32x4{lo[0], lo[1], hi[0], hi[1]};
}
__device__ __forceinline__ void sum_rows_put(const float (&a)[24], float* dst, int lane, float scale) {
  float b[12], c[6];
#pragma unroll
  for (int k = 0; k < 12; ++k) b[k] = tr32(a[k], a[k + 12]);
#pragma unroll
  for (int k = 0; k < 6; ++k) c[k] = tr16(b[k], b[k + 6]);
#pragma unroll
  for (int k = 0; k < 6; ++k) c[k] += dpp<ROR8>(c[k]);
  const int i = lane & 7, dup = (lane >> 3) & 1, a0 = 12 * (lane >> 5) + 6 * ((lane >> 4) & 1) + 3 * dup;
#pragma unroll
  for (int k = 0; k < 3; ++k) { const int a_ = a0 + k; dst[64 * (a_ >> 3) + 8 * i + (a_ & 7)] = sel(dup, c[3 + k], c[k]) * scale; }
}

__global__ void __launch_bounds__(NT, 3) fused_decode(const u16* __restrict__ x_, const u16* __restrict__ kc_, const u16* __restrict__ vc_,
    const u16* __restrict__ wq_, const u16* __restrict__ wo_, const u16* __restrict__ w1_, const u16* __restrict__ w2_, u16* __restrict__ out_, unsigned char* ws_) {
  __shared__ __attribute__((aligned(16))) Lds L;
  asm volatile("" :: "s"(x_), "s"(kc_), "s"(vc_), "s"(wq_), "s"(wo_), "s"(w1_), "s"(w2_), "s"(out_), "s"(ws_));
  const int tid = threadIdx.x, lane = tid & 63, w = __builtin_amdgcn_readfirstlane(tid >> 6);
  const int blk = blockIdx.x, h = blk & 31, m = blk >> 5;
  const int r = lane >> 3, i = lane & 7;
  const bool is1 = !(h & 1) && (m == 5 || (m == 7 && h >= 16));
  const int j1 = m == 5 ? h >> 1 : 8 + (h >> 1);
  const int role = m < 5 ? 0 : (is1 ? 1 : ((m == 6 || (m == 7 && h < 8)) ? 2 : 3));
  if (tid < 52) L.cnt[tid] = 0u;
  __syncthreads();

  if (w < 4) {
    const u32 lo0 = r * 384 + i * 16, lo1 = lo0 + 128, lo2 = lo0 + 256;
    const u32 hb = (u32)h * (SK * 384) + (u32)(m * 4 + w) * 3072;
    constexpr u32 TSTR = 32 * 3072, BSTR = (u32)H * SK * 384;
    const __amdgpu_buffer_rsrc_t rk = __builtin_amdgcn_make_buffer_rsrc((void*)kc_, 0, 2 * BSTR, 0x00020000);
    const __amdgpu_buffer_rsrc_t rv = __builtin_amdgcn_make_buffer_rsrc((void*)vc_, 0, 2 * BSTR, 0x00020000);
    auto ld3 = [&](u32x4 (&d)[3], __amdgpu_buffer_rsrc_t rt, u32 so) {
      d[0] = __builtin_amdgcn_raw_buffer_load_b128(rt, lo0, so, 2); d[1] = __builtin_amdgcn_raw_buffer_load_b128(rt, lo1, so, 2); d[2] = __builtin_amdgcn_raw_buffer_load_b128(rt, lo2, so, 2);
    };
    u32x4 pre[PB][3], ring[3][3];
    SLEEP(SDELAY);
#pragma unroll
    for (int n = 0; n < PB + 3; ++n) {
      if (n >= 2) asm volatile("s_waitcnt vmcnt(3)" ::: "memory");
      SB();
      if (n < PB) ld3(pre[n], rk, hb + n * TSTR); else ld3(ring[n - PB], rk, hb + n * TSTR);
      SB();
    }
    lds_wait<1>(&L.cnt[C_Q], 3);
    u32x4 qf[3];
#pragma unroll
    for (int t = 0; t < 3; ++t) qf[t] = *(const u32x4*)&L.q[0][64 * t + 8 * i];
    float* scw = &L.sc[w][0][0] + r;
    u32 so = hb + (PB + 3) * TSTR;
    int mode = 0, nck = CK0, ck = 0;
    auto throttle = [&]() {
      SB();
      asm volatile("s_waitcnt vmcnt(%1)\n\ts_cmp_gt_i32 %0, 1\n\ts_cbranch_scc1 1f\n\ts_waitcnt vmcnt(%2)\n\ts_cmp_gt_i32 %0, 0\n\ts_cbranch_scc1 1f\n\ts_waitcnt vmcnt(%3)\n\t"
                   "s_cmp_gt_i32 %0, -1\n\ts_cbranch_scc1 1f\n\ts_waitcnt vmcnt(%4)\n\ts_cmp_gt_i32 %0, -2\n\ts_cbranch_scc1 1f\n\ts_waitcnt vmcnt(%5)\n1:"
                   :: "s"(mode), "n"(8), "n"(6), "n"(5), "n"(4), "n"(3) : "memory", "scc");
      SB();
    };
    auto refill = [&](int slot, __amdgpu_buffer_rsrc_t rt, bool jump) {
      throttle(); ring[slot][0] = __builtin_amdgcn_raw_buffer_load_b128(rt, lo0, so, 2);
      throttle(); ring[slot][1] = __builtin_amdgcn_raw_buffer_load_b128(rt, lo1, so, 2);
      throttle(); ring[slot][2] = __builtin_amdgcn_raw_buffer_load_b128(rt, lo2, so, 2);
      so += jump ? BSTR - 31 * TSTR : TSTR;
      SB();
    };
    auto setmode = [&](int now, i32x2 sc, int n) {
      const int A = __builtin_amdgcn_readfirstlane(sc[0]), B = __builtin_amdgcn_readfirstlane(sc[1]);
      if (n == nck) { *(volatile LAS int*)&L.tck[ck][w] = now | 1; ++ck; nck += CKS; }
      mode = B == 0 ? 0 : (now - A - (n * B >> 8) + (1 << (DSH - 1))) >> DSH;
    };
    auto kdot = [&](const u32x4 (&d)[3]) -> float {
      float s0 = dot2(d[0][0], qf[0][0], 0.f), s1 = dot2(d[1][0], qf[1][0], 0.f), s2 = dot2(d[2][0], qf[2][0], 0.f);
#pragma unroll
      for (int k = 1; k < 4; ++k) { s0 = dot2(d[0][k], qf[0][k], s0); s1 = dot2(d[1][k], qf[1][k], s1); s2 = dot2(d[2][k], qf[2][k], s2); }
      float s = (s0 + s1) + s2;
      asm volatile("" : "+v"(s));
      return s;
    };
    constexpr int KP = 3 + (64 - PB) % 3;
#pragma unroll
    for (int n = PB; n < PB + KP; ++n) { const float s = kdot(ring[(n - PB) % 3]); refill((n - PB) % 3, rk, false); scw[n * 8] = sum8lo(s); }
    SB();
#pragma unroll
    for (int n = 0; n < PB; ++n) scw[n * 8] = sum8lo(kdot(pre[n]));
    SB();
    static_assert((PB + KP) % 3 == 1 && CK0 % 3 == 1 && CKS % 3 == 0 && CK0 >= PB + KP && TRIG % 3 == 1 && (127 - CK0) / CKS < 8 && CK0 + (NCK - 1) * CKS > 96 && CK0 + ((64 - CK0) / CKS + 1) * CKS < 90 && (61 - CK0) % CKS != 0 && CK0 + (NCK - 1) * CKS < 124, "loop phases");
#pragma unroll 1
    for (int n = PB + KP; n < 61; n += 3) {
      const int now = (int)__builtin_amdgcn_s_memrealtime();
      const i32x2 sc = *(volatile LAS i32x2*)L.sched;
      SB();
      const float s0 = kdot(ring[KP % 3]);
      setmode(now, sc, n);
      refill(KP % 3, rk, n == 28);
      if (n == 31) {
#pragma unroll
        for (int t = 0; t < 3; ++t) qf[t] = *(const u32x4*)&L.q[1][64 * t + 8 * i];
      }
      const float s1 = kdot(ring[(KP + 1) % 3]);
      refill((KP + 1) % 3, rk, false);
      const float s2 = kdot(ring[(KP + 2) % 3]);
      refill((KP + 2) % 3, rk, false);
      scw[n * 8] = sum8lo(s0); scw[n * 8 + 8] = sum8lo(s1); scw[n * 8 + 16] = sum8lo(s2);
    }
    so = hb;
#pragma unroll
    for (int u = 0; u < 3; ++u) { const float s = kdot(ring[(KP + u) % 3]); refill((KP + u) % 3, rv, false); scw[(61 + u) * 8] = sum8lo(s); }
    float mw[2];
    CBAR();
#pragma unroll
    for (int b = 0; b < 2; ++b) {
      f32x4 s = *(const f32x4*)&L.sc[w][b][4 * lane];
#pragma unroll
      for (int k = 0; k < 4; ++k) s[k] = rbf(rbf(s[k]) * 0.07216878364870322f);
      mw[b] = wave_max(fmaxf(fmaxf(s[0], s[1]), fmaxf(s[2], s[3])));
#pragma unroll
      for (int k = 0; k < 4; ++k) s[k] = __expf(s[k] - mw[b]);
      const u32 p01 = pk_bf16(s[0], s[1]), p23 = pk_bf16(s[2], s[3]);
      *(u32x4*)&L.sc[w][b][4 * lane] = u32x4{p01 & 0xffffu, p01 >> 16, p23 & 0xffffu, p23 >> 16};
      const float lw = wave_sum((s[0] + s[1]) + (s[2] + s[3]));
      if (lane == 0) { L.mx[b][w] = mw[b]; L.ls[b][w] = lw; }
    }
    if (lane == 0) lds_arrive(&L.cnt[C_ML]);
    CBAR();
    float acc[24];
#pragma unroll
    for (int k = 0; k < 24; ++k) acc[k] = 0.f;
    float f1 = 0.f;
    const u32* pw = (const u32*)scw;
    auto vacc = [&](const u32x4 (&d)[3], u32 plo) {
      const u32 phi = plo << 16;
#pragma unroll
      for (int t = 0; t < 3; ++t)
#pragma unroll
        for (int k = 0; k < 4; ++k) { acc[8 * t + 2 * k] = dot2(d[t][k], plo, acc[8 * t + 2 * k]); acc[8 * t + 2 * k + 1] = dot2(d[t][k], phi, acc[8 * t + 2 * k + 1]); }
#pragma unroll
      for (int t = 0; t < 3; ++t)
        asm volatile("" : "+v"(acc[8 * t + 7]), "+v"(acc[8 * t + 6]), "+v"(acc[8 * t + 5]), "+v"(acc[8 * t + 4]), "+v"(acc[8 * t + 3]), "+v"(acc[8 * t + 2]), "+v"(acc[8 * t + 1]), "+v"(acc[8 * t]));
    };
    u32 p0 = pw[0], p1 = pw[8], p2 = pw[16];
#pragma unroll 1
    for (int n = 64; n < 124; n += 3) {
      const int now = (int)__builtin_amdgcn_s_memrealtime();
      const i32x2 sc = *(volatile LAS i32x2*)L.sched;
      const u32 q0 = pw[(n - 61) * 8], q1 = pw[(n - 60) * 8], q2 = pw[(n - 59) * 8];
      SB();
      vacc(ring[KP % 3], p0);
      setmode(now, sc, n);
      refill(KP % 3, rv, false);
      vacc(ring[(KP + 1) % 3], p1);
      refill((KP + 1) % 3, rv, n == 91);
      if (n == 94) {
        lds_wait<1>(&L.cnt[C_GML], 1);
        const f32x4 g = *(const f32x4*)&L.gml[0];
        const float f0 = __expf(mw[0] - g[0]) * g[2];
        f1 = __expf(mw[1] - g[1]) * g[3];
        sum_rows_put(acc, L.red[w][0], lane, f0);
        if (lane == 0) lds_arrive(&L.cnt[C_V0]);
#pragma unroll
        for (int k = 0; k < 24; ++k) acc[k] = 0.f;
      }
      vacc(ring[(KP + 2) % 3], p2);
      refill((KP + 2) % 3, rv, false);
      p0 = q0; p1 = q1; p2 = q2;
      if ((n == TRIG || n == TRIG - 6) && lane == 0) lds_arrive(&L.cnt[C_TRIG]);
    }
    const __amdgpu_buffer_rsrc_t rs = __builtin_amdgcn_make_buffer_rsrc((void*)ws_, 0, WS_END, 0x00020000);
    const u32 tag = __builtin_amdgcn_readfirstlane(*(volatile LAS u32*)&L.cnt[C_TAG]);
    int sp_l = (lane < 48 ? lane : 0) * 2, sp_g = lane < 48 ? WS_PA + blk * 3072 + 1536 + lane * 16 : WS_END;
    PIN(sp_l); PIN(sp_g);
    {
      u32x4 last[3];
      ld3(last, rv, so);
      const u32 p3 = pw[63 * 8];
      SB();
      vacc(ring[KP % 3], p0);
      vacc(ring[(KP + 1) % 3], p1);
      vacc(ring[(KP + 2) % 3], p2);
      vacc(last, p3);
    }
    {
      sum_rows_put(acc, L.red[w][1], lane, f1);
      const u32 old = lds_add_relaxed(&L.cnt[C_V1], lane);
      f32x2 v = (*(const f32x2*)&L.red[0][1][sp_l] + *(const f32x2*)&L.red[1][1][sp_l]) + (*(const f32x2*)&L.red[2][1][sp_l] + *(const f32x2*)&L.red[3][1][sp_l]);
      f32x2 u = (*(const f32x2*)&L.red[0][1][sp_l + 96] + *(const f32x2*)&L.red[1][1][sp_l + 96]) + (*(const f32x2*)&L.red[2][1][sp_l + 96] + *(const f32x2*)&L.red[3][1][sp_l + 96]);
      PIN(v); PIN(u);
      if (__builtin_amdgcn_readfirstlane(old) == 3u) { STSC1(f2bits(v[0]), f2bits(v[1]), sp_g); STSC1(f2bits(u[0]), f2bits(u[1]), sp_g + 768); }
    }
    __syncthreads();
    return;
  }

  const int a = w - 4;
  const GAS unsigned char* xg = (const GAS unsigned char*)x_;
  const __amdgpu_buffer_rsrc_t rs = __builtin_amdgcn_make_buffer_rsrc((void*)ws_, 0, WS_END, 0x00020000);
  u32 tag;
  if (a < 5) {
    if (m < 4 || m > 5) {
      const int qt = 24 * (blk & 7) + (m < 4 ? m : m - 2) * 4 + (h >> 3);
      const int kh = qt >= 96 ? 1 : 0, sidx = qt - 96 * kh, e0 = 160 * kh + 32 * a;
      u32 xr[2][5];
      u32x4 wqv[4][2];
#pragma unroll
      for (int b = 0; b < 2; ++b)
#pragma unroll
        for (int j = 0; j < 5; ++j) xr[b][j] = *(const GAS u32*)(xg + b * (D * 2) + (64 * j + lane) * 4);
      const GAS unsigned char* wqb = (const GAS unsigned char*)wq_ + (size_t)(2 * e0) * (HD * 2) + sidx * 128;
      const u32 lo2 = r * (2 * HD * 2) + i * 16;
#pragma unroll
      for (int g = 0; g < 4; ++g)
#pragma unroll
        for (int c = 0; c < 2; ++c) wqv[g][c] = ldnt(wqb + (size_t)(16 * g + c) * (HD * 2) + lo2);
      const u32 ep = __builtin_amdgcn_raw_buffer_load_b32(rs, WS_EPOCH, 0, 0);
      SB();
      float s0 = 0.f, s1 = 0.f;
#pragma unroll
      for (int j = 0; j < 5; ++j) { s0 = dot2(xr[0][j], 0x3f803f80u, s0); s1 = dot2(xr[1][j], 0x3f803f80u, s1); }
      float s = tr32(s0, s1); s = tr16(s, s); s += dpp<ROR8>(s); s = sum8lo(s);
      s = s / (float)D;
      const float mean[2] = {rdlane(s, 0), rdlane(s, 32)};
      float q0 = 0.f, q1 = 0.f;
#pragma unroll
      for (int j = 0; j < 5; ++j) {
        const float d0 = bflo(xr[0][j]) - mean[0], d1 = bfhi(xr[0][j]) - mean[0], d2 = bflo(xr[1][j]) - mean[1], d3 = bfhi(xr[1][j]) - mean[1];
        q0 = fmaf(d0, d0, q0); q0 = fmaf(d1, d1, q0); q1 = fmaf(d2, d2, q1); q1 = fmaf(d3, d3, q1);
      }
      float q = tr32(q0, q1); q = tr16(q, q); q += dpp<ROR8>(q); q = sum8lo(q);
      const float dn = rbf(sqrtf(rbf(rbf(q / (float)D) + 1e-5f)));
      u32 xs[2][4];
      int j0 = e0 >> 6; asm volatile("" : "+v"(j0));
#pragma unroll
      for (int b = 0; b < 2; ++b) {
        u32 sl = xr[b][4];
#pragma unroll
        for (int j = 0; j < 4; ++j) sl = f2bits(sel(j0 == j, bits2f(xr[b][j]), bits2f(sl)));
        const float mb = rbf(mean[b]), den = rdlane(dn, 32 * b);
        const u32 dd = pk_bf16(bflo(sl) - mb, bfhi(sl) - mb);
        sl = pk_bf16(bflo(dd) / den, bfhi(dd) / den);
#pragma unroll
        for (int g = 0; g < 4; ++g) xs[b][g] = __shfl(sl, (e0 & 63) + 8 * g + r);
      }
      float a0[8], a1[8];
#pragma unroll
      for (int k = 0; k < 8; ++k) { a0[k] = 0.f; a1[k] = 0.f; }
#pragma unroll
      for (int g = 0; g < 4; ++g)
#pragma unroll
        for (int k = 0; k < 4; ++k) {
          const u32 plo = __builtin_amdgcn_perm(wqv[g][1][k], wqv[g][0][k], 0x05040100u);
          const u32 phi = __builtin_amdgcn_perm(wqv[g][1][k], wqv[g][0][k], 0x07060302u);
          a0[2 * k] = dot2(plo, xs[0][g], a0[2 * k]); a0[2 * k + 1] = dot2(phi, xs[0][g], a0[2 * k + 1]);
          a1[2 * k] = dot2(plo, xs[1][g], a1[2 * k]); a1[2 * k + 1] = dot2(phi, xs[1][g], a1[2 * k + 1]);
        }
      float c1[8], c2[4];
#pragma unroll
      for (int k = 0; k < 8; ++k) c1[k] = tr32(a0[k], a1[k]);
#pragma unroll
      for (int k = 0; k < 4; ++k) c2[k] = tr16(c1[k], c1[k + 4]);
#pragma unroll
      for (int k = 0; k < 4; ++k) c2[k] += dpp<ROR8>(c2[k]);
      *(f32x4*)&L.redq[a][lane >> 5][8 * i + 4 * ((lane >> 4) & 1)] = f32x4{c2[0], c2[1], c2[2], c2[3]};
      u32 old = 0;
      if (lane == 0) old = lds_arrive(&L.cnt[C_QP]);
      tag = __builtin_amdgcn_readfirstlane(ep) + 1u;
      if (__builtin_amdgcn_readfirstlane(old) == 4u) {
        float v0 = 0.f, v1 = 0.f;
#pragma unroll
        for (int ww = 0; ww < 5; ++ww) { v0 += L.redq[ww][0][lane]; v1 += L.redq[ww][1][lane]; }
        STSC1(f2bits(v0), f2bits(v1), WS_QA + qt * 1024 + lane * 16);
      }
    } else {
      tag = __builtin_amdgcn_readfirstlane(__builtin_amdgcn_raw_buffer_load_b32(rs, WS_EPOCH, 0, 0)) + 1u;
    }
  } else {
    tag = __builtin_amdgcn_readfirstlane(__builtin_amdgcn_raw_buffer_load_b32(rs, WS_EPOCH, 0, 0)) + 1u;
    const int off = WS_QA + (h * 3 + a - 5) * 1024 + lane * 16;
    u32x4 g0, g1;
    SLEEP(QSLEEP0);
    for (u32 spins = 0;; ++spins) {
      CBAR();
      g0 = LDSC1(off); g1 = LDSC1(off + 96 * 1024);
      if (__all((BADTAG(g0) | BADTAG(g1)) == 0) || spins > SPIN_CAP) break;
      SLEEP(QSLEEP);
    }
    const u32 qq = pk_bf16(bits2f(g0[0]) + bits2f(g1[0]), bits2f(g0[2]) + bits2f(g1[2]));
    L.q[0][64 * (a - 5) + lane] = (u16)qq; L.q[1][64 * (a - 5) + lane] = (u16)(qq >> 16);
    if (lane == 0) { L.cnt[C_TAG] = tag; lds_arrive(&L.cnt[C_Q]); }
  }
  unsigned char* wi = (unsigned char*)L.wimg;

  if (a == 7) {
    constexpr int KML = (64 - CK0) / CKS;
    int Tprev = 0;
#pragma unroll 1
    for (int k = 0; k < NCK; ++k) {
      if (CK0 + k * CKS > 96 && CK0 + (k - 1) * CKS <= 96) {
        lds_wait<16>(&L.cnt[C_V0], 4);
#pragma unroll
        for (int u = 0; u < 2; ++u) {
          const int j = 48 * u + (lane < 48 ? lane : 0);
          const f32x2 v = (*(const f32x2*)&L.red[0][0][2 * j] + *(const f32x2*)&L.red[1][0][2 * j]) + (*(const f32x2*)&L.red[2][0][2 * j] + *(const f32x2*)&L.red[3][0][2 * j]);
          STSC1(f2bits(v[0]), f2bits(v[1]), lane < 48 ? WS_PA + blk * 3072 + j * 16 : WS_END);
        }
      }
      i32x4 t;
#pragma nounroll
      for (u32 sp = 0; sp < LDS_CAP; ++sp) { t = *(volatile LAS i32x4*)&L.tck[k][0]; if (t[0] && t[1] && t[2] && t[3]) break; SLEEP(16); }
      const int mine = t[0] + ((t[1] - t[0]) + (t[2] - t[0]) + (t[3] - t[0])) / 4;
      if (lane == 0 && (h >> 3) == (m & 3)) __builtin_amdgcn_raw_buffer_store_b64(u32x2{(u32)mine, tag}, rs, WS_CK + k * 512 + (m * 8 + (h & 7)) * 8, 0, 16);
      if (k == KML) {
        lds_wait<16>(&L.cnt[C_ML], 4);
        const int b = lane >> 5;
        const f32x4 mv = *(const f32x4*)&L.mx[b][0], lv = *(const f32x4*)&L.ls[b][0];
        const float M = fmaxf(fmaxf(mv[0], mv[1]), fmaxf(mv[2], mv[3]));
        const float Ls = (lv[0] * __expf(mv[0] - M) + lv[1] * __expf(mv[1] - M)) + (lv[2] * __expf(mv[2] - M) + lv[3] * __expf(mv[3] - M));
        const float own = (lane & 1) ? Ls : M;
        auto sw = __builtin_amdgcn_permlane32_swap(f2bits(own), f2bits(own), false, false);
        STSC1(sw[0], sw[1], lane < 2 ? WS_ML + blk * 128 + lane * 16 : WS_END);
      }
      if (k == KML + 1) {
        u32x4 g;
        for (u32 spins = 0;; ++spins) {
          CBAR();
          g = LDSC1(WS_ML + (h + 32 * i) * 128 + (r & 1) * 16);
          if (__all(BADTAG(g) == 0) || spins > SPIN_CAP) break;
          SLEEP(32);
        }
        float res[4];
#pragma unroll
        for (int bb = 0; bb < 2; ++bb) {
          const float o1 = bits2f(g[2 * bb]), o2 = dpp<ROR8>(o1);
          const float mp = (r & 1) ? o2 : o1, lp = (r & 1) ? o1 : o2;
          res[bb] = max8lo(mp);
          res[2 + bb] = 1.f / sum8lo(lp * __expf(mp - res[bb]));
        }
        if (lane == 0) { *(f32x4*)&L.gml[0] = f32x4{res[0], res[1], res[2], res[3]}; lds_arrive(&L.cnt[C_GML]); }
      }
      const int due = __builtin_amdgcn_readfirstlane(mine) + CKWAIT;
#pragma nounroll
      for (int sp = 0; sp < 256 && (int)__builtin_amdgcn_s_memrealtime() - due < 0; ++sp) SLEEP(16);
      float sm = 0.f, cn = 0.f;
#pragma nounroll
      for (int tr = 0; tr < 2; ++tr) {
        CBAR();
        const u32x2 g = __builtin_amdgcn_raw_buffer_load_b64(rs, WS_CK + k * 512 + lane * 8, 0, 16);
        const bool ok = g[1] == tag;
        cn = (float)__popcll(__ballot(ok));
        sm = wave_sum(ok ? (float)((int)g[0] - mine) : 0.f);
        if (cn >= 56.f) break;
        SLEEP(100);
      }
      const int T = mine + (cn > 0.f ? (int)(sm / cn) : 0);
      if (k >= 1) {
        const int B = (T - Tprev) * AGG / CKS;
        if (lane == 0) *(volatile LAS i32x2*)L.sched = i32x2{T - ((CK0 + k * CKS) * B >> 8), B};
      }
      Tprev = T;
    }
  }

  if (role == 0) {
    int off = WS_PA + (h + 32 * i) * 3072 + a * 256 + r * 16;
    int ao = 16 * a + r + 8 * (i >> 2), ar = (lane & 1) * ACT_S + 8 * (lane >> 4), yo = lane < 16 ? WS_PY + (h * D + 128 * m + 16 * a + lane) * 16 : WS_END;
    PIN(off); PIN(ao); PIN(ar); PIN(yo);
    auto gather_o = [&](int b) {
      u32x4 g0 = LDSC1(off + b * 1536), g1 = LDSC1(off + b * 1536 + 128);
#pragma nounroll
      for (u32 n_ = SPIN_CAP; n_; --n_) {
        const u64 ms = MISS(g0) | MISS(g1);
        if (ms == 0) break;
        if (__popcll(ms) > 16) SLEEP(FAR); else SLEEP(NEAR);
        CBAR();
        if ((BADTAG(g0) | BADTAG(g1)) != 0) { g0 = LDSC1(off + b * 1536); g1 = LDSC1(off + b * 1536 + 128); }
      }
      float u0 = bits2f(g0[0]), u1 = bits2f(g0[2]), v0 = bits2f(g1[0]), v1 = bits2f(g1[2]);
      u0 += dpp<HMIR>(u0); u1 += dpp<HMIR>(u1); v0 += dpp<HMIR>(v0); v1 += dpp<HMIR>(v1);
      float x0 = (i & 4) ? v0 : u0, x1 = (i & 4) ? v1 : u1;
      x0 += dpp<XOR1>(x0); x1 += dpp<XOR1>(x1);
      x0 += dpp<XOR2>(x0); x1 += dpp<XOR2>(x1);
      ((u32*)L.act[b])[ao] = pk_bf16(x0, x1);
    };
    u32x4 tw[6];
    const GAS unsigned char* p = (const GAS unsigned char*)wo_ + (size_t)(192 * h + 24 * a + (lane >> 4)) * (D * 2) + m * 256 + (lane & 15) * 16;
    if (a == 0 || a == 6) {
      lds_wait<64>(&L.cnt[C_V0], 1);
      u32 t = 0;
      if (lane == 0) t = __builtin_nontemporal_load((const GAS u32*)p);
      asm volatile("" :: "v"(t));
    }
    lds_wait<48>(&L.cnt[C_TRIG], 1); lds_wait<4>(&L.cnt[C_TRIG], 8);
#pragma unroll
    for (int k = 0; k < 6; ++k) tw[k] = ldnt(p + k * (4 * D * 2));
    SB();
    __syncthreads();
    if (a < 6) gather_o(0);
    SB();
#pragma unroll
    for (int k = 0; k < 6; ++k) *(u32x4*)(wi + (24 * a + 4 * k + (lane >> 4)) * 288 + (lane & 15) * 16) = tw[k];
    if (a < 6) gather_o(1);
    int bo = (8 * (lane >> 4) + ((lane >> 2) & 3)) * 288 + a * 32 + (lane & 3) * 8;
    __syncthreads();
    f32x4 d = {0.f, 0.f, 0.f, 0.f};
#pragma unroll
    for (int ks = 0; ks < 6; ++ks) d = mfma(*(const u32x4*)&L.act[0][ar + 32 * ks], bfrag(wi + bo + 32 * ks * 288, 4 * 288), d);
    STSC1(f2bits(d[0]), f2bits(d[1]), yo);
  } else if (role == 1) {
    u32x4 b1[20] = {}, b2[10];
    if (a < 7) {
      lds_wait<64>(&L.cnt[C_Q], 3);
      for (int k = 0; k <= a; ++k) SLEEP(12);
      const GAS unsigned char* p2 = (const GAS unsigned char*)w2_ + (size_t)(64 * j1) * (D * 2) + lane * 16;
#pragma unroll 1
      for (int k = a; k < 80; k += 7) { const u32x4 v = ldnt(p2 + k * 1024); *(u32x4*)(wi + k * 1024 + lane * 16) = v; SLEEP(PACE); }
      for (int e = a * 64 + lane; e < ACT_S / 2; e += 448) ((u32*)L.act[2])[e] = 0x3f803f80u;
      if (lane == 0) lds_arrive(&L.cnt[C_W2]);
    }
    lds_wait<32>(&L.cnt[C_W2], 7);
#pragma unroll
    for (int f = 0; f < 10; ++f) { b2[f] = bfrag(wi + (32 * (f & 1) + 8 * (lane >> 4) + ((lane >> 2) & 3)) * 1280 + (5 * a + (f >> 1)) * 32 + (lane & 3) * 8, 4 * 1280); asm volatile("" : "+v"(b2[f])); }
    int ho = WS_PH + (lane + 64 * a) * 16, hl = lane + 64 * a, ar = ((lane & 2) ? 2 : (lane & 1)) * ACT_S + 8 * (lane >> 4);
    int a2w = (lane >> 4 & 1) * 96 + 16 * a + (lane & 15), a2r = (lane & 1) * 96 + 8 * (lane >> 4);
    int z0 = WS_PZ + (j1 * D + 80 * a + lane) * 16, z1 = lane < 16 ? WS_PZ + (j1 * D + 80 * a + 64 + lane) * 16 : WS_END;
    PIN(ho); PIN(hl); PIN(ar); PIN(a2w); PIN(a2r); PIN(z0); PIN(z1);
    {
      const GAS unsigned char* p = (const GAS unsigned char*)w1_ + (size_t)(80 * a + r) * (F * 2) + j1 * 128 + i * 16;
      u32x4 tw[10];
      if (a < 7) {
        lds_wait<64>(&L.cnt[C_V0], 1);
        u32 t = 0;
        if (lane == 0) t = __builtin_nontemporal_load((const GAS u32*)(p + (a == 6 ? (size_t)159 * (F * 2) : 0)));
        asm volatile("" :: "v"(t));
      }
      lds_wait<16>(&L.cnt[C_TRIG], 1); lds_wait<4>(&L.cnt[C_TRIG], 8);
#pragma unroll
      for (int k = 0; k < 10; ++k) tw[k] = ldnt(p + (size_t)(8 * k) * (F * 2));
      SB();
      __syncthreads();
#pragma unroll
      for (int k = 0; k < 10; ++k) *(u32x4*)(wi + (10 * a + k) * 1024 + lane * 16) = tw[k];
      __syncthreads();
      if (a < 4) {
#pragma unroll
        for (int ks = 0; ks < 20; ++ks) { b1[ks] = bfrag(wi + (32 * ks + 8 * (lane >> 4) + ((lane >> 2) & 3)) * 128 + a * 32 + (lane & 3) * 8, 4 * 128); asm volatile("" : "+v"(b1[ks])); }
      }
    }
    SLEEP(D4);
    if (a == 7) {
      u32x4 g0, g1;
      const int s0 = WS_PS + (lane >> 1) * 128 + (lane & 1) * 16, s1 = lane < 16 ? s0 + 4096 : WS_END;
#pragma nounroll
      for (u32 n_ = SPIN_CAP; n_; --n_) {
        CBAR();
        g0 = LDSC1(s0); g1 = LDSC1(s1);
        if ((MISS(g0) | (MISS(g1) & 0xffffull)) == 0) break;
        SLEEP(NEAR);
      }
      float v = tr32(bits2f(g0[0]) + bits2f(g1[0]), bits2f(g0[2]) + bits2f(g1[2]));
      v = tr16(v, v); v += dpp<ROR8>(v); v += dpp<ROR4>(v); v += dpp<XOR2>(v);
      v *= 1.f / D;
      const float ot = dpp<XOR1>(v);
      const float rd = __builtin_amdgcn_rcpf(rbf(__builtin_amdgcn_sqrtf(rbf(rbf(fmaxf(fmaf(-v, v, ot), 0.f)) + 1e-5f))));
      if ((lane & 31) == 0) *(f32x2*)&L.st[lane >> 4] = f32x2{rbf(v), rd};
      lds_add_relaxed(&L.cnt[C_ST], lane);
    } else if (a < 5) {
      u32x4 g;
      POLL1(g, ho);
      ((u32*)L.act[0])[hl] = g[0]; ((u32*)L.act[1])[hl] = g[2];
      lds_add_relaxed(&L.cnt[C_H], lane);
      if (a < 4) {
#pragma nounroll
        for (u32 n_ = LDS_CAP; n_ && *(volatile LAS u32*)&L.cnt[C_H] < 5u; --n_) {}
        CBAR();
        f32x4 d0 = {0.f, 0.f, 0.f, 0.f}, d1 = d0;
#pragma unroll
        for (int ks = 0; ks < 20; ks += 2) {
          d0 = mfma(*(const u32x4*)&L.act[0][ar + 32 * ks], b1[ks], d0);
          d1 = mfma(*(const u32x4*)&L.act[0][ar + 32 * ks + 32], b1[ks + 1], d1);
        }
        f32x4 st;
#pragma nounroll
        for (u32 n_ = LDS_CAP; n_; --n_) { const u32 f = *(volatile LAS u32*)&L.cnt[C_ST]; st = *(volatile LAS f32x4*)&L.st[0]; if (f) break; }
        d0 += d1;
        const u32 pv = pk_bf16((d0[0] - st[0] * d0[2]) * st[1], (d0[1] - st[2] * d0[2]) * st[3]);
        const float v = (lane & 16) ? bfhi(pv) : bflo(pv);
        const float sv = v * __builtin_amdgcn_rcpf(1.f + __expf(-v));
        L.act2[0][a2w] = (u16)pk_bf16(sv, sv);
      }
    }
    __syncthreads();
    const u32x4 a0 = *(const u32x4*)&L.act2[0][a2r], a1 = *(const u32x4*)&L.act2[0][a2r + 32];
    f32x4 z[5];
#pragma unroll
    for (int t = 0; t < 5; ++t) z[t] = mfma(a0, b2[2 * t], f32x4{0.f, 0.f, 0.f, 0.f});
#pragma unroll
    for (int t = 0; t < 5; ++t) z[t] = mfma(a1, b2[2 * t + 1], z[t]);
    const int g4 = lane >> 4;
    const float s0 = sel(g4 & 2, sel(g4 & 1, z[3][0], z[2][0]), sel(g4 & 1, z[1][0], z[0][0]));
    const float s1 = sel(g4 & 2, sel(g4 & 1, z[3][1], z[2][1]), sel(g4 & 1, z[1][1], z[0][1]));
    STSC1(f2bits(s0), f2bits(s1), z0);
    STSC1(f2bits(z[4][0]), f2bits(z[4][1]), z1);
  } else {
    const int k5 = (m - 6) * 32 + h, c = lane & 15;
    u32 xv = 0;
    if (role == 2) xv = *(const GAS u16*)(xg + (16 * k5 + c) * 2) | ((u32)*(const GAS u16*)(xg + (D + 16 * k5 + c) * 2) << 16);
    int o3 = ((4 * a + (lane >> 4)) * D + 16 * k5 + c) * 16, rw = (a * 16 + c) * 2 + (lane >> 5), rc = c;
    int oh = (lane & 49) == 0 ? WS_PH + (8 * k5 + (c >> 1)) * 16 : WS_END, os = (lane & 44) == 0 ? WS_PS + k5 * 128 + (lane & 3) * 32 + (lane & 16) : WS_END;
    int oo = role == 2 && (lane & 33) == 0 ? ((lane >> 4) * D + 16 * k5 + c) * 2 : 2 * D * 2;
    float x0 = bflo(xv), x1 = bfhi(xv);
    PIN(o3); PIN(rw); PIN(rc); PIN(oh); PIN(os); PIN(oo); PIN(x0); PIN(x1);
    if (role == 2 && a == 0) {
      lds_wait<64>(&L.cnt[C_V0], 1);
      u32 t = 0;
      if (lane == 0) t = __builtin_nontemporal_load((const GAS u32*)out_ + 8 * k5);
      asm volatile("" :: "v"(t));
    }
    __syncthreads();
    if (role == 2) {
      u32x4 g;
      SLEEP(D3);
      POLL1(g, WS_PY + o3);
      float v = tr32(bits2f(g[0]), bits2f(g[2]));
      v = tr16(v, v);
      ((float*)L.r3)[rw] = v;
      u32 old = lds_add_relaxed(&L.cnt[C_S3], lane);
      f32x2 t[8];
#pragma unroll
      for (int u = 0; u < 8; ++u) t[u] = L.r3[u][rc];
      f32x2 ts = ((t[0] + t[1]) + (t[2] + t[3])) + ((t[4] + t[5]) + (t[6] + t[7]));
      PIN(ts);
      if (__builtin_amdgcn_readfirstlane(old) == 7u) {
        const u32 at = pk_bf16(ts[0], ts[1]);
        const u32 hh = pk_bf16(x0 + bflo(at), x1 + bfhi(at));
        const u32 hx = f2bits(dpp<XOR1>(bits2f(hh)));
        STSC1(__builtin_amdgcn_perm(hx, hh, 0x05040100u), __builtin_amdgcn_perm(hx, hh, 0x07060302u), oh);
        float s0 = bflo(hh), s1 = bfhi(hh);
        if (lane & 16) { s0 *= s0; s1 *= s1; }
        s0 = sum8lo(s0); s1 = sum8lo(s1); s0 += dpp<ROR8>(s0); s1 += dpp<ROR8>(s1);
        STSC1(f2bits(s0), f2bits(s1), os);
        ((u32*)L.act2)[rc] = hh;
      }
      __syncthreads();
      if (a < 6) {
        const u32 hh = ((const u32*)L.act2)[rc];
        float hb = (lane & 16) ? bfhi(hh) : bflo(hh);
        int rb = rc * 2 + ((lane >> 4) & 1);
        PIN(hb); PIN(rb);
        const __amdgpu_buffer_rsrc_t ro = __builtin_amdgcn_make_buffer_rsrc((void*)out_, 0, 2 * D * 2, 0x00020000);
        SLEEP(D5);
        POLL1(g, WS_PZ + o3);
        float z = tr32(bits2f(g[0]), bits2f(g[2]));
        z = tr16(z, z);
        ((float*)L.r3)[rw] = z;
        old = lds_add_relaxed(&L.cnt[C_S5], lane);
        float q[6];
#pragma unroll
        for (int u = 0; u < 6; ++u) q[u] = ((const float*)L.r3)[u * 32 + rb];
        const float res = hb + rbf(((q[0] + q[1]) + (q[2] + q[3])) + (q[4] + q[5]));
        u32 rp = pk_bf16(res, dpp<XOR1>(res));
        PIN(rp);
        if (__builtin_amdgcn_readfirstlane(old) == 5u) {
          __builtin_amdgcn_raw_buffer_store_b32(rp, ro, oo, 0, 16);
          if (k5 == 0 && lane == 0) __builtin_amdgcn_raw_buffer_store_b32(tag, rs, WS_EPOCH, 0, 16);
        }
      }
    }
  }
}
}

extern "C" void kernel_launch(void* const* d_in, const int* in_sizes, int n_in, void* d_out, int out_size, void* d_ws, size_t ws_size, hipStream_t stream) {
  hipLaunchKernelGGL(fk::fused_decode, dim3(fk::NWG), dim3(fk::NT), 0, stream, (const u16*)d_in[0], (const u16*)d_in[1], (const u16*)d_in[2],
                     (const u16*)d_in[3], (const u16*)d_in[4], (const u16*)d_in[6], (const u16*)d_in[7], (u16*)d_out, (unsigned char*)d_ws);
}
```
